```python
import torch

def _softmax(x, axis=-1):
    return torch.softmax(x, dim=axis)
def _gelu(x):
    return torch.nn.functional.gelu(x, approximate="tanh")
def _silu(x):
    return torch.nn.functional.silu(x)
def _sigmoid(x):
    return torch.sigmoid(x)
def _layernorm(x, axis=-1, eps=1e-5):
    m = x.mean(dim=axis, keepdim=True)
    v = x.var(dim=axis, keepdim=True, unbiased=False)
    return (x - m) / torch.sqrt(v + eps)
def _scatter_add(tgt, idx, src, axis=0):
    return tgt.clone().index_add_(axis, idx, src)
def _segment_sum(src, seg, n):
    out = torch.zeros((n,) + src.shape[1:], dtype=src.dtype)
    return out.index_add_(0, seg, src)
def _pad(x, pw):
    flat = tuple(v for pair in reversed(pw) for v in pair)
    return torch.nn.functional.pad(x, flat)

kernel_name = 'standalone__batchnorm__prompt'

def setup_inputs():

    _w = (lambda a: (lambda t: t.to(torch.bfloat16) if t.is_floating_point() else t)(torch.as_tensor(a)))
    B, S, C = 2, 2048, 576
    K = 3
    return {
        "x": _w((lambda *s: torch.randn(*s, dtype=torch.bfloat16) * 0.05)(B, S, C)),
        "w": _w((lambda *s: torch.randn(*s, dtype=torch.bfloat16) * 0.05)(K, C, C) * (1.0 / (K * C) ** 0.5)),
        "mean": _w((lambda *s: torch.randn(*s, dtype=torch.bfloat16) * 0.05)(C) * 0.1),
        "var": _w(torch.abs((lambda *s: torch.randn(*s, dtype=torch.bfloat16) * 0.05)(C)) + 0.5),
        "g": _w((lambda *s: torch.randn(*s, dtype=torch.bfloat16) * 0.05)(C) + 1.0),
        "b": _w((lambda *s: torch.randn(*s, dtype=torch.bfloat16) * 0.05)(C) * 0.1),
    }

def reference(x, w, mean, var, g, b):
    K = 3
    P = K // 2
    S = x.shape[1]
    xp = _pad(x, ((0, 0), (P, P), (0, 0)))
    y = sum(torch.einsum('bsc,cd->bsd', xp[:, k:k + S, :], w[k]) for k in range(K))
    inv_std = 1.0 / torch.sqrt(var + 1e-5)
    y = (y - mean) * (g * inv_std) + b
    return _silu(y)
```

```cpp
#include <hip/hip_runtime.h>
#include <hip/hip_bf16.h>

__global__ void naive_kernel(const __hip_bfloat16* __restrict__ x, const __hip_bfloat16* __restrict__ w,
                             const __hip_bfloat16* __restrict__ mean, const __hip_bfloat16* __restrict__ var,
                             const __hip_bfloat16* __restrict__ g, const __hip_bfloat16* __restrict__ b,
                             __hip_bfloat16* __restrict__ out, int B, int S, int C) {
  int idx = blockIdx.x * blockDim.x + threadIdx.x;
  int total = B * S * C;
  if (idx >= total) return;
  int d = idx % C;
  int m = idx / C;
  int s = m % S;
  int bb = m / S;
  float acc = 0.f;
  for (int k = 0; k < 3; ++k) {
    int ss = s + k - 1;
    if (ss < 0 || ss >= S) continue;
    const __hip_bfloat16* xr = x + ((size_t)bb * S + ss) * C;
    const __hip_bfloat16* wk = w + (size_t)k * C * C;
    float a = 0.f;
    for (int c = 0; c < C; ++c) a += __bfloat162float(xr[c]) * __bfloat162float(wk[(size_t)c * C + d]);
    acc += a;
  }
  float inv_std = 1.0f / sqrtf(__bfloat162float(var[d]) + 1e-5f);
  float y = (acc - __bfloat162float(mean[d])) * (__bfloat162float(g[d]) * inv_std) + __bfloat162float(b[d]);
  float r = y / (1.0f + __expf(-y));
  out[idx] = __float2bfloat16(r);
}

extern "C" void kernel_launch(void* const* d_in, const int* in_sizes, int n_in,
                              void* d_out, int out_size, void* d_ws, size_t ws_size,
                              hipStream_t stream) {
  const __hip_bfloat16* x = (const __hip_bfloat16*)d_in[0];
  const __hip_bfloat16* w = (const __hip_bfloat16*)d_in[1];
  const __hip_bfloat16* mean = (const __hip_bfloat16*)d_in[2];
  const __hip_bfloat16* var = (const __hip_bfloat16*)d_in[3];
  const __hip_bfloat16* g = (const __hip_bfloat16*)d_in[4];
  const __hip_bfloat16* b = (const __hip_bfloat16*)d_in[5];
  __hip_bfloat16* out = (__hip_bfloat16*)d_out;
  int C = in_sizes[2];
  int B = 2, S = in_sizes[0] / (B * C);
  int total = out_size;
  naive_kernel<<<(total + 255) / 256, 256, 0, stream>>>(x, w, mean, var, g, b, out, B, S, C);
}
```

```cpp
#include <hip/hip_runtime.h>
#include <hip/hip_bf16.h>
#include <stdint.h>

typedef __attribute__((ext_vector_type(8))) short bf16x8;
typedef __attribute__((ext_vector_type(4))) short s16x4;
typedef __attribute__((ext_vector_type(4))) float f32x4;
typedef __attribute__((ext_vector_type(2))) unsigned u32x2;
typedef __attribute__((address_space(3))) char* lds_ptr;
typedef __attribute__((address_space(3))) const char* lds_cptr;

namespace cfg {
constexpr int C = 576;
constexpr int S = 2048;
constexpr int ROWB = C * 2;
constexpr int BM = 128;
constexpr int XROWS = BM + 2;
constexpr int KC = 64;
constexpr int NCC = C / KC;
constexpr int XT_BYTES = XROWS * 128;
constexpr int RING = 3;
}
using namespace cfg;

template <int NB> struct Geo {
  static constexpr int RG = NB * 256;
  static constexpr int WT_BYTES = 8 * RG;
  static constexpr int SLOT = XT_BYTES + 3 * WT_BYTES;
  static constexpr int WINS = (NB * 16 * 4 + 63) / 64;
  static constexpr int LOADS = 5 + 3 * ((NB == 5) ? 3 : 2);
};
constexpr int LDS_BYTES = RING * Geo<5>::SLOT;

#define WAIT_V(n) asm volatile("s_waitcnt vmcnt(%0)" ::"n"(n) : "memory")

__device__ __forceinline__ float bf2f(unsigned short u) { return __uint_as_float(((unsigned)u) << 16); }
__device__ __forceinline__ unsigned pack_bf16x2(float lo, float hi) {
  unsigned r; asm("v_cvt_pk_bf16_f32 %0, %1, %2" : "=v"(r) : "v"(lo), "v"(hi)); return r;
}

template <int NB>
__device__ __forceinline__ void tile_body(const unsigned short* __restrict__ x, const unsigned short* __restrict__ w,
                                          const unsigned short* __restrict__ mean, const unsigned short* __restrict__ var,
                                          const unsigned short* __restrict__ gam, const unsigned short* __restrict__ bet,
                                          unsigned short* __restrict__ out, int m0, int n0, char* smem) {
  using G = Geo<NB>;
  const int tid = threadIdx.x, lane = tid & 63;
  const int wave = __builtin_amdgcn_readfirstlane(tid >> 6);
  const int li = lane & 15, g = lane >> 4;

  const int s0 = m0 % S;
  const bool first = (s0 == 0), last = (s0 + BM == S);
  auto rsX = __builtin_amdgcn_make_buffer_rsrc((void*)(x + (long)(m0 - 1) * C), 0, XROWS * ROWB, 0x00020000);
  auto rsW = __builtin_amdgcn_make_buffer_rsrc((void*)(w + n0), 0, 3 * C * ROWB - n0 * 2, 0x00020000);

  unsigned voffX[5];
#pragma unroll
  for (int i = 0; i < 5; ++i) {
    int j = (i < 4) ? ((i * 4 + wave) * 64 + lane) : (1024 + wave * 4 + (lane & 3));
    int rho = j >> 3, cph = j & 7, c = cph ^ (rho & 6);
    unsigned v = (unsigned)(rho * ROWB + c * 16);
    bool bad = (first && rho == 0) || (last && rho == XROWS - 1);
    voffX[i] = bad ? 0x7FFFFFF0u : v;
  }
  constexpr int WPW = NB * 16 * 4 * 2 / 4;
  unsigned voffW[3];
#pragma unroll
  for (int i = 0; i < 3; ++i) {
    int j = wave * WPW + i * 64 + lane;
    int rg = j / (NB * 16), rem = j % (NB * 16);
    int cb = rem >> 4, idx = rem & 15, pos = idx >> 1, half = idx & 1;
    int rin = pos ^ ((rg & 1) << 2);
    int row = rg * 8 + rin;
    voffW[i] = (unsigned)(row * ROWB + cb * 32 + half * 16);
  }
  lds_ptr lds = (lds_ptr)smem;

  auto issue = [&](int cc, int sl) {
    lds_ptr base = lds + sl * G::SLOT;
#pragma unroll
    for (int i = 0; i < 4; ++i)
      __builtin_amdgcn_raw_ptr_buffer_load_lds(rsX, base + (i * 4 + wave) * 1024, 16, voffX[i], cc * 128, 0, 0);
    if (lane < 4)
      __builtin_amdgcn_raw_ptr_buffer_load_lds(rsX, base + 16384 + wave * 64, 16, voffX[4], cc * 128, 0, 0);
#pragma unroll
    for (int t = 0; t < 3; ++t) {
      lds_ptr wb = base + XT_BYTES + t * G::WT_BYTES + wave * (WPW * 16);
      int so = (t * C + cc * KC) * ROWB;
      __builtin_amdgcn_raw_ptr_buffer_load_lds(rsW, wb, 16, voffW[0], so, 0, 0);
      __builtin_amdgcn_raw_ptr_buffer_load_lds(rsW, wb + 1024, 16, voffW[1], so, 0, 0);
      if (NB == 5) {
        if (lane < 32) __builtin_amdgcn_raw_ptr_buffer_load_lds(rsW, wb + 2048, 16, voffW[2], so, 0, 0);
      }
    }
  };

  unsigned xaddr[3][2];
#pragma unroll
  for (int t = 0; t < 3; ++t)
#pragma unroll
    for (int ks = 0; ks < 2; ++ks) {
      int rho = 32 * wave + li + t, c = 4 * ks + g;
      xaddr[t][ks] = (unsigned)(rho * 128 + ((c ^ (rho & 6)) << 4));
    }
  unsigned waddr[2];
#pragma unroll
  for (int tt = 0; tt < 2; ++tt)
    waddr[tt] = (unsigned)(g * G::RG + ((tt ^ (g & 1)) * 128) + ((li >> 2) * 32) + ((li & 3) * 8));

  float scl[NB][4], sft[NB][4];
#pragma unroll
  for (int nb = 0; nb < NB; ++nb) {
    int n = n0 + 16 * nb + 4 * g;
    s16x4 mv = *(const s16x4*)(mean + n), vv = *(const s16x4*)(var + n), gv = *(const s16x4*)(gam + n), bv = *(const s16x4*)(bet + n);
#pragma unroll
    for (int r = 0; r < 4; ++r) {
      float inv = 1.0f / sqrtf(bf2f((unsigned short)vv[r]) + 1e-5f);
      float sc = bf2f((unsigned short)gv[r]) * inv;
      scl[nb][r] = sc;
      sft[nb][r] = bf2f((unsigned short)bv[r]) - bf2f((unsigned short)mv[r]) * sc;
    }
  }

  f32x4 acc[2][NB];
#pragma unroll
  for (int mb = 0; mb < 2; ++mb)
#pragma unroll
    for (int nb = 0; nb < NB; ++nb) acc[mb][nb] = (f32x4){0.f, 0.f, 0.f, 0.f};

  issue(0, 0);
  issue(1, 1);

#pragma unroll
  for (int cc = 0; cc < NCC; ++cc) {
    if (cc + 1 < NCC) WAIT_V(G::LOADS); else WAIT_V(0);
    __builtin_amdgcn_s_barrier();
    if (cc + 2 < NCC) issue(cc + 2, (cc + 2) % RING);
    const int sl = cc % RING;
    lds_cptr xb = (lds_cptr)lds + sl * G::SLOT;
    lds_cptr wbb = xb + XT_BYTES;
#pragma unroll
    for (int t = 0; t < 3; ++t) {
#pragma unroll
      for (int ks = 0; ks < 2; ++ks) {
        bf16x8 bfr[2];
#pragma unroll
        for (int mb = 0; mb < 2; ++mb)
          bfr[mb] = *(const __attribute__((address_space(3))) bf16x8*)(xb + xaddr[t][ks] + mb * 2048);
        bf16x8 afr[NB];
#pragma unroll
        for (int nb = 0; nb < NB; ++nb) {
          lds_cptr p0 = wbb + t * G::WT_BYTES + ks * 4 * G::RG + nb * 256;
          s16x4 lo = __builtin_amdgcn_ds_read_tr16_b64_v4i16((__attribute__((address_space(3))) s16x4*)(p0 + waddr[0]));
          s16x4 hi = __builtin_amdgcn_ds_read_tr16_b64_v4i16((__attribute__((address_space(3))) s16x4*)(p0 + waddr[1]));
          afr[nb] = __builtin_shufflevector(lo, hi, 0, 1, 2, 3, 4, 5, 6, 7);
        }
#pragma unroll
        for (int mb = 0; mb < 2; ++mb)
#pragma unroll
          for (int nb = 0; nb < NB; ++nb)
            acc[mb][nb] = __builtin_amdgcn_mfma_f32_16x16x32_bf16(afr[nb], bfr[mb], acc[mb][nb], 0, 0, 0);
      }
    }
  }

#pragma unroll
  for (int mb = 0; mb < 2; ++mb) {
    int m = m0 + 32 * wave + 16 * mb + li;
    unsigned short* orow = out + (long)m * C + n0 + 4 * g;
#pragma unroll
    for (int nb = 0; nb < NB; ++nb) {
      float v[4];
#pragma unroll
      for (int r = 0; r < 4; ++r) {
        float y = acc[mb][nb][r] * scl[nb][r] + sft[nb][r];
        v[r] = y * __builtin_amdgcn_rcpf(1.0f + __expf(-y));
      }
      u32x2 pk = {pack_bf16x2(v[0], v[1]), pack_bf16x2(v[2], v[3])};
      *(u32x2*)(orow + nb * 16) = pk;
    }
  }
}

extern "C" __global__ __launch_bounds__(256, 1)
void conv_bn_silu_kernel(const unsigned short* __restrict__ x, const unsigned short* __restrict__ w,
                         const unsigned short* __restrict__ mean, const unsigned short* __restrict__ var,
                         const unsigned short* __restrict__ gam, const unsigned short* __restrict__ bet,
                         unsigned short* __restrict__ out) {
  __shared__ __attribute__((aligned(16))) char smem[LDS_BYTES];
  int bid = blockIdx.x;
  int xcd = bid & 7, idx = bid >> 3;
  int mt = 8 * (xcd & 3) + (idx >> 2);
  int nt = 4 * (xcd >> 2) + (idx & 3);
  int m0 = mt * BM;
  int n0 = 144 * (nt >> 1) + 80 * (nt & 1);
  if (nt & 1) tile_body<4>(x, w, mean, var, gam, bet, out, m0, n0, smem);
  else        tile_body<5>(x, w, mean, var, gam, bet, out, m0, n0, smem);
}

extern "C" void kernel_launch(void* const* d_in, const int* in_sizes, int n_in,
                              void* d_out, int out_size, void* d_ws, size_t ws_size,
                              hipStream_t stream) {
  const unsigned short* x = (const unsigned short*)d_in[0];
  const unsigned short* w = (const unsigned short*)d_in[1];
  const unsigned short* mean = (const unsigned short*)d_in[2];
  const unsigned short* var = (const unsigned short*)d_in[3];
  const unsigned short* gam = (const unsigned short*)d_in[4];
  const unsigned short* bet = (const unsigned short*)d_in[5];
  unsigned short* out = (unsigned short*)d_out;
  conv_bn_silu_kernel<<<256, 256, 0, stream>>>(x, w, mean, var, gam, bet, out);
}
```

```cpp
#include <hip/hip_runtime.h>
#include <hip/hip_bf16.h>
#include <stdint.h>

typedef __attribute__((ext_vector_type(8))) short bf16x8;
typedef __attribute__((ext_vector_type(4))) short s16x4;
typedef __attribute__((ext_vector_type(4))) float f32x4;
typedef __attribute__((ext_vector_type(4))) unsigned u32x4;
typedef __attribute__((address_space(3))) char* lds_ptr;
typedef __attribute__((address_space(3))) const char* lds_cptr;

constexpr int C = 576;
constexpr int S = 2048;
constexpr int ROWB = C * 2;
constexpr int BM = 160;
constexpr int OUTR = BM - 2;
constexpr int MT_PER_B = (S + OUTR - 1) / OUTR;
constexpr int NG = C / 64;
constexpr int NWG = 2 * MT_PER_B * NG;
constexpr int KC = 64;
constexpr int NCC = C / KC;
constexpr int XT_BYTES = BM * 128;
constexpr int WT_BYTES = 64 * 128;
constexpr int SLOT = 3 * WT_BYTES + XT_BYTES;
constexpr int RING = 3;
constexpr int LDS_BYTES = RING * SLOT + 128;
constexpr int LOADS = 5 + 6;
constexpr int NSTEP = NCC * 6;

#define WAIT_V(n) asm volatile("s_waitcnt vmcnt(%0)" ::"n"(n) : "memory")
#define WAIT_L0() asm volatile("s_waitcnt lgkmcnt(0)" ::: "memory")
#define SCHED() __builtin_amdgcn_sched_barrier(0)

__device__ __forceinline__ float bf2f(unsigned short u) { return __uint_as_float(((unsigned)u) << 16); }
__device__ __forceinline__ unsigned pack_bf16x2(float lo, float hi) {
  unsigned r; asm("v_cvt_pk_bf16_f32 %0, %1, %2" : "=v"(r) : "v"(lo), "v"(hi)); return r;
}
__device__ __forceinline__ void dma16(u32x4 rsrc, unsigned voff, unsigned soff, unsigned lds_dst) {
  asm volatile("s_mov_b32 m0, %0\n\ts_nop 0\n\tbuffer_load_dwordx4 %1, %2, %3 offen lds"
               :: "s"(lds_dst), "v"(voff), "s"(rsrc), "s"(soff) : "memory");
}
__device__ __forceinline__ u32x4 make_rsrc(const void* base, unsigned bytes) {
  uint64_t a = (uint64_t)base;
  u32x4 r = {(unsigned)a, (unsigned)(a >> 32) & 0xFFFFu, bytes, 0x00020000u};
  r[0] = __builtin_amdgcn_readfirstlane(r[0]); r[1] = __builtin_amdgcn_readfirstlane(r[1]);
  r[2] = __builtin_amdgcn_readfirstlane(r[2]); r[3] = __builtin_amdgcn_readfirstlane(r[3]);
  return r;
}

#define TSARG

extern "C" __global__ __launch_bounds__(256, 1)
void conv_bn_silu_kernel(const unsigned short* __restrict__ x, const unsigned short* __restrict__ w,
                         const unsigned short* __restrict__ mean, const unsigned short* __restrict__ var,
                         const unsigned short* __restrict__ gam, const unsigned short* __restrict__ bet,
                         unsigned short* __restrict__ out TSARG) {
  __shared__ __attribute__((aligned(16))) char smem[LDS_BYTES];
  int bid = blockIdx.x;
  {
    constexpr int q = NWG / 8, r = NWG % 8;
    int xcd = bid & 7, off = bid >> 3;
    bid = (xcd < r ? xcd * (q + 1) : r * (q + 1) + (xcd - r) * q) + off;
  }
  int ngb = bid / (3 * 2 * MT_PER_B), rem = bid % (3 * 2 * MT_PER_B);
  int mt = rem / 3, ng = ngb * 3 + rem % 3;
  int bb = mt / MT_PER_B, j = mt % MT_PER_B;
  const int s0 = j * OUTR - 1;
  const int n0 = ng * 64;

  const int tid = threadIdx.x, lane = tid & 63;
  const int wave = __builtin_amdgcn_readfirstlane(tid >> 6);
  const int li = lane & 15, g = lane >> 4;
  const int wm = wave >> 1, wn = wave & 1;

  const u32x4 rsX = make_rsrc(x + ((long)bb * S + s0) * C, BM * ROWB);
  const u32x4 rsW = make_rsrc(w + n0, 3 * C * ROWB - n0 * 2);

  unsigned voffX[5];
#pragma unroll
  for (int i = 0; i < 5; ++i) {
    int q = wave * 5 + i;
    int rho = 8 * q + (lane >> 3), cph = lane & 7, c = cph ^ (rho & 6);
    int s = s0 + rho;
    unsigned v = (unsigned)(rho * ROWB + c * 16);
    voffX[i] = (s < 0 || s >= S) ? 0x7FFFFFF0u : v;
  }
  unsigned voffW[2];
#pragma unroll
  for (int i = 0; i < 2; ++i) {
    int rg = wave * 2 + i;
    int nbp = lane >> 4, idx = lane & 15, pos = idx >> 1, half = idx & 1;
    int row = rg * 8 + (pos ^ ((rg & 1) << 2));
    voffW[i] = (unsigned)(row * ROWB + nbp * 32 + half * 16);
  }
  const unsigned lds0 = (unsigned)(size_t)(lds_ptr)smem;
  const unsigned xdst = lds0 + 3 * WT_BYTES + wave * 5 * 1024;
  const unsigned wdst = lds0 + wave * 2 * 1024;

  auto issue_piece = [&](int cc, int sl, int p) {
    if (p < 5) dma16(rsX, voffX[p], cc * 128, xdst + sl * SLOT + p * 1024);
    else { int t = (p - 5) >> 1, i = (p - 5) & 1;
      dma16(rsW, voffW[i], (t * C + cc * KC) * ROWB, wdst + sl * SLOT + t * WT_BYTES + i * 1024); }
  };
  auto issue = [&](int cc, int sl) {
#pragma unroll
    for (int p = 0; p < LOADS; ++p) issue_piece(cc, sl, p);
  };

  issue(0, 0);
  issue(1, 1);
  issue(2, 2);

  lds_ptr lds = (lds_ptr)smem;
  unsigned xaddr[3][2];
#pragma unroll
  for (int t = 0; t < 3; ++t)
#pragma unroll
    for (int ks = 0; ks < 2; ++ks) {
      int rho = 80 * wm + li + t - 1, c = 4 * ks + g;
      xaddr[t][ks] = (unsigned)(3 * WT_BYTES + rho * 128 + ((c ^ (rho & 6)) << 4));
    }
  unsigned waddr[2];
#pragma unroll
  for (int tt = 0; tt < 2; ++tt)
    waddr[tt] = (unsigned)(g * 1024 + wn * 512 + ((tt ^ (g & 1)) * 128) + ((li >> 2) * 32) + ((li & 3) * 8));

  f32x4 acc[5][2];
#pragma unroll
  for (int mb = 0; mb < 5; ++mb)
#pragma unroll
    for (int nb = 0; nb < 2; ++nb) acc[mb][nb] = (f32x4){0.f, 0.f, 0.f, 0.f};

  bf16x8 xf[2][5];
  s16x4 wlo[2][2], whi[2][2];
  auto read_one = [&](int step, int buf, int r) {
    const int cc = step / 6, t = (step % 6) >> 1, ks = step & 1, sl = cc % RING;
    lds_cptr sb = (lds_cptr)lds + sl * SLOT;
    lds_cptr wp = sb + t * WT_BYTES + ks * 4096;
    lds_cptr xp = sb + xaddr[t][ks];
    switch (r) {
      case 0: wlo[buf][0] = __builtin_amdgcn_ds_read_tr16_b64_v4i16((__attribute__((address_space(3))) s16x4*)(wp + waddr[0])); break;
      case 1: whi[buf][0] = __builtin_amdgcn_ds_read_tr16_b64_v4i16((__attribute__((address_space(3))) s16x4*)(wp + waddr[1])); break;
      case 2: xf[buf][0] = *(const __attribute__((address_space(3))) bf16x8*)(xp); break;
      case 3: wlo[buf][1] = __builtin_amdgcn_ds_read_tr16_b64_v4i16((__attribute__((address_space(3))) s16x4*)(wp + 256 + waddr[0])); break;
      case 4: whi[buf][1] = __builtin_amdgcn_ds_read_tr16_b64_v4i16((__attribute__((address_space(3))) s16x4*)(wp + 256 + waddr[1])); break;
      default: xf[buf][r - 4] = *(const __attribute__((address_space(3))) bf16x8*)(xp + (r - 4) * 2048); break;
    }
  };

  WAIT_V(2 * LOADS);
  __builtin_amdgcn_s_barrier();
#pragma unroll
  for (int r = 0; r < 9; ++r) read_one(0, 0, r);

#pragma unroll
  for (int step = 0; step < NSTEP; ++step) {
    const int cur = step & 1;
    const int cc = step / 6;
    const bool boundary = (step % 6 == 5) && (step + 1 < NSTEP);
    SCHED();
    if (boundary) {
      WAIT_L0();
      if (cc + 2 < NCC) WAIT_V(LOADS); else WAIT_V(0);
      __builtin_amdgcn_s_barrier();
    }
    bf16x8 wf0 = __builtin_shufflevector(wlo[cur][0], whi[cur][0], 0, 1, 2, 3, 4, 5, 6, 7);
    bf16x8 wf1 = __builtin_shufflevector(wlo[cur][1], whi[cur][1], 0, 1, 2, 3, 4, 5, 6, 7);
#pragma unroll
    for (int i = 0; i < 10; ++i) {
      const int mb = i >> 1, nb = i & 1;
      acc[mb][nb] = __builtin_amdgcn_mfma_f32_16x16x32_bf16(nb ? wf1 : wf0, xf[cur][mb], acc[mb][nb], 0, 0, 0);
      if (step + 1 < NSTEP && i < 9) read_one(step + 1, cur ^ 1, i);
      {
        const int bcc = boundary ? cc : cc - 1;
        const int u = boundary ? 0 : (step % 6) + 1;
        if (bcc >= 0 && bcc + 3 < NCC && u < 6) {
          int p = -1;
          if (i == 3) p = 2 * u; else if (i == 7) p = 2 * u + 1;
          if (p >= 0 && p < LOADS) issue_piece(bcc + 3, bcc % RING, p);
        }
      }
      SCHED();
    }
  }

  float scl[2][4], sft[2][4];
#pragma unroll
  for (int nb = 0; nb < 2; ++nb) {
    int n = n0 + 32 * wn + 16 * nb + 4 * g;
    s16x4 mv = *(const s16x4*)(mean + n), vv = *(const s16x4*)(var + n), gv = *(const s16x4*)(gam + n), bv = *(const s16x4*)(bet + n);
#pragma unroll
    for (int r = 0; r < 4; ++r) {
      float inv = 1.0f / sqrtf(bf2f((unsigned short)vv[r]) + 1e-5f);
      float sc = bf2f((unsigned short)gv[r]) * inv;
      scl[nb][r] = sc;
      sft[nb][r] = bf2f((unsigned short)bv[r]) - bf2f((unsigned short)mv[r]) * sc;
    }
  }
#pragma unroll
  for (int mb = 0; mb < 5; ++mb) {
    int rho = 80 * wm + 16 * mb + li;
    int s = s0 + rho;
    bool ok = (rho >= 1) && (rho <= OUTR) && (s < S);
    unsigned pk[2][2];
#pragma unroll
    for (int nb = 0; nb < 2; ++nb) {
      float v[4];
#pragma unroll
      for (int r = 0; r < 4; ++r) {
        float y = acc[mb][nb][r] * scl[nb][r] + sft[nb][r];
        v[r] = y * __builtin_amdgcn_rcpf(1.0f + __expf(-y));
      }
      pk[nb][0] = pack_bf16x2(v[0], v[1]);
      pk[nb][1] = pack_bf16x2(v[2], v[3]);
    }
    auto r0v = __builtin_amdgcn_permlane16_swap(pk[0][0], pk[1][0], false, false);
    auto r1v = __builtin_amdgcn_permlane16_swap(pk[0][1], pk[1][1], false, false);
    u32x4 o = {r0v[0], r1v[0], r0v[1], r1v[1]};
    if (ok) {
      unsigned short* p = out + ((long)bb * S + s) * C + n0 + 32 * wn + 16 * (g & 1) + 8 * (g >> 1);
      *(u32x4*)p = o;
    }
  }
}

extern "C" void kernel_launch(void* const* d_in, const int* in_sizes, int n_in,
                              void* d_out, int out_size, void* d_ws, size_t ws_size,
                              hipStream_t stream) {
  const unsigned short* x = (const unsigned short*)d_in[0];
  const unsigned short* w = (const unsigned short*)d_in[1];
  const unsigned short* mean = (const unsigned short*)d_in[2];
  const unsigned short* var = (const unsigned short*)d_in[3];
  const unsigned short* gam = (const unsigned short*)d_in[4];
  const unsigned short* bet = (const unsigned short*)d_in[5];
  unsigned short* out = (unsigned short*)d_out;
  conv_bn_silu_kernel<<<NWG, 256, 0, stream>>>(x, w, mean, var, gam, bet, out);
}
```

```cpp
#include <hip/hip_runtime.h>
#include <hip/hip_bf16.h>
#include <stdint.h>
#define ABL 0
#define UNROLL 1
#define ROT 1

typedef __attribute__((ext_vector_type(8))) short bf16x8;
typedef __attribute__((ext_vector_type(4))) short s16x4;
typedef __attribute__((ext_vector_type(4))) float f32x4;
typedef __attribute__((ext_vector_type(4))) unsigned u32x4;
typedef __attribute__((address_space(3))) char* lds_ptr;
typedef __attribute__((address_space(3))) const char* lds_cptr;

constexpr int C = 576;
constexpr int S = 2048;
constexpr int ROWB = C * 2;
constexpr int BM = 160;
constexpr int OUTR = BM - 2;
constexpr int MT_PER_B = (S + OUTR - 1) / OUTR;
constexpr int NG = C / 64;
constexpr int NWG = 2 * MT_PER_B * NG;
constexpr int KC = 64;
constexpr int NCC = C / KC;
constexpr int XT_BYTES = BM * 128;
constexpr int WT_BYTES = 64 * 128;
constexpr int SLOT = 3 * WT_BYTES + XT_BYTES;
constexpr int RING = 3;
constexpr int LDS_BYTES = RING * SLOT + 128;
constexpr int LOADS = 5 + 6;

#define WAIT_V(n) asm volatile("s_waitcnt vmcnt(%0)" ::"n"(n) : "memory")
#define WAIT_L0() asm volatile("s_waitcnt lgkmcnt(0)" ::: "memory")
#define SCHED() __builtin_amdgcn_sched_barrier(0)

__device__ __forceinline__ float bf2f(unsigned short u) { return __uint_as_float(((unsigned)u) << 16); }
__device__ __forceinline__ unsigned pack_bf16x2(float lo, float hi) {
  unsigned r; asm("v_cvt_pk_bf16_f32 %0, %1, %2" : "=v"(r) : "v"(lo), "v"(hi)); return r;
}
__device__ __forceinline__ void dma16(u32x4 rsrc, unsigned voff, unsigned soff, unsigned lds_dst) {
  asm volatile("s_mov_b32 m0, %0\n\ts_nop 0\n\tbuffer_load_dwordx4 %1, %2, %3 offen lds"
               :: "s"(lds_dst), "v"(voff), "s"(rsrc), "s"(soff) : "memory");
}
__device__ __forceinline__ u32x4 make_rsrc(const void* base, unsigned bytes) {
  uint64_t a = (uint64_t)base;
  u32x4 r = {(unsigned)a, (unsigned)(a >> 32) & 0xFFFFu, bytes, 0x00020000u};
  r[0] = __builtin_amdgcn_readfirstlane(r[0]); r[1] = __builtin_amdgcn_readfirstlane(r[1]);
  return r;
}

#define TS_WAVES 8
#define TSARG
#define STAMP(v)

extern "C" __global__ __launch_bounds__(512, 2)
void conv_bn_silu_kernel(const unsigned short* __restrict__ x, const unsigned short* __restrict__ w,
                         const unsigned short* __restrict__ mean, const unsigned short* __restrict__ var,
                         const unsigned short* __restrict__ gam, const unsigned short* __restrict__ bet,
                         unsigned short* __restrict__ out TSARG) {
  __shared__ __attribute__((aligned(16))) char smem[LDS_BYTES];
  int bid = blockIdx.x;
  {
    constexpr int q = NWG / 8, r = NWG % 8;
    int xcd = bid & 7, off = bid >> 3;
    bid = (xcd < r ? xcd * (q + 1) : r * (q + 1) + (xcd - r) * q) + off;
  }
  int ngb = bid / (3 * 2 * MT_PER_B), rem = bid % (3 * 2 * MT_PER_B);
  int mt = rem / 3, ngi = rem % 3, ng = ngb * 3 + ngi;
  int bb = mt / MT_PER_B, j = mt % MT_PER_B;
  const int s0 = j * OUTR - 1;
  const int n0 = ng * 64;

  const int tid = threadIdx.x, lane = tid & 63;
  const int wave = __builtin_amdgcn_readfirstlane(tid >> 6);
  const unsigned lds0 = (unsigned)(size_t)(lds_ptr)smem;

  const u32x4 rsX = make_rsrc(x + (long)bb * S * C, S * ROWB);
  const u32x4 rsW = make_rsrc(w + n0, 3 * C * ROWB - n0 * 2);

  if (wave >= 4) {
    const int lw = wave - 4;
    unsigned voffX[5];
    {
      const int srow = s0 + 40 * lw + (lane >> 3);
      const int v0 = srow * ROWB + (((lane & 7) ^ ((lane >> 3) & 6)) << 4);
#pragma unroll
      for (int p = 0; p < 5; ++p) voffX[p] = ((unsigned)(srow + 8 * p) < (unsigned)S) ? (unsigned)(v0 + p * 8 * ROWB) : 0x7FFFFFF0u;
    }
    unsigned voffW[2];
#pragma unroll
    for (int i = 0; i < 2; ++i) {
      int rg = lw * 2 + i;
      int nbp = lane >> 4, idx = lane & 15, pos = idx >> 1, half = idx & 1;
      int row = rg * 8 + (pos ^ ((rg & 1) << 2));
      voffW[i] = (unsigned)(row * ROWB + nbp * 32 + half * 16);
    }
    const unsigned xdst = lds0 + 3 * WT_BYTES + lw * 5 * 1024;
    const unsigned wdst = lds0 + lw * 2 * 1024;
    int ccr = 0;
    const int rot0 = 1 + ((mt + 3 * ngi) & 7);
    unsigned sl = 0;
    auto issue = [&]() {
      const unsigned so = ccr * 128;
#pragma unroll
      for (int p = 0; p < 5; ++p) dma16(rsX, voffX[p], so, xdst + sl + p * 1024);
#pragma unroll
      for (int t = 0; t < 3; ++t)
#pragma unroll
        for (int i = 0; i < 2; ++i) dma16(rsW, voffW[i], (unsigned)(t * C * ROWB) + (unsigned)ccr * (KC * ROWB), wdst + sl + t * WT_BYTES + i * 1024);
      if (ROT) ccr = (ccr == 0) ? rot0 : ((ccr == NCC - 1) ? 1 : ccr + 1); else ccr = ccr + 1;
      sl = (sl == 2 * SLOT) ? 0 : sl + SLOT;
    };
    STAMP(pa);
    issue();
    issue();
    STAMP(pb);
    WAIT_V(LOADS);
    STAMP(pc);
    __builtin_amdgcn_s_barrier();
    issue();
    for (int jb = 0; jb < NCC - 1; ++jb) {
      if (jb + 2 < NCC) WAIT_V(LOADS); else WAIT_V(0);
      __builtin_amdgcn_s_barrier();
      if (jb + 3 < NCC) issue();
    }
    STAMP(pd);
    return;
  }

  const int li = lane & 15, g = lane >> 4;
  const int wm = wave >> 1, wn = wave & 1;
  lds_ptr lds = (lds_ptr)smem;

  float scl[2][4], sft[2][4], scl2[2][4], sft2[2][4];
#pragma unroll
  for (int nb = 0; nb < 2; ++nb) {
    int n = n0 + 32 * wn + 16 * nb + 4 * g;
    s16x4 mv = *(const s16x4*)(mean + n), vv = *(const s16x4*)(var + n), gv = *(const s16x4*)(gam + n), bv = *(const s16x4*)(bet + n);
#pragma unroll
    for (int r = 0; r < 4; ++r) {
      float sc = bf2f((unsigned short)gv[r]) * __builtin_amdgcn_rsqf(bf2f((unsigned short)vv[r]) + 1e-5f);
      float sf = bf2f((unsigned short)bv[r]) - bf2f((unsigned short)mv[r]) * sc;
      scl[nb][r] = sc; sft[nb][r] = sf;
      scl2[nb][r] = sc * -1.4426950408889634f; sft2[nb][r] = sf * -1.4426950408889634f;
    }
  }

  unsigned xaddr[3][2];
#pragma unroll
  for (int t = 0; t < 3; ++t)
#pragma unroll
    for (int ks = 0; ks < 2; ++ks) {
      int rho = 80 * wm + li + t - 1, c = 4 * ks + g;
      xaddr[t][ks] = (unsigned)(3 * WT_BYTES + rho * 128 + ((c ^ (rho & 6)) << 4));
    }
  unsigned waddr[2];
#pragma unroll
  for (int tt = 0; tt < 2; ++tt)
    waddr[tt] = (unsigned)(g * 1024 + wn * 512 + ((tt ^ (g & 1)) * 128) + ((li >> 2) * 32) + ((li & 3) * 8));

  f32x4 acc[5][2];
#pragma unroll
  for (int mb = 0; mb < 5; ++mb)
#pragma unroll
    for (int nb = 0; nb < 2; ++nb) acc[mb][nb] = (f32x4){0.f, 0.f, 0.f, 0.f};

  bf16x8 xf[2][5];
  s16x4 wlo[2][2], whi[2][2];
  auto read_one = [&](lds_cptr sb, int st, int buf, int r) {
    const int t = st >> 1, ks = st & 1;
    lds_cptr wp = sb + t * WT_BYTES + ks * 4096;
    lds_cptr xp = sb + xaddr[t][ks];
    switch (r) {
      case 0: wlo[buf][0] = __builtin_amdgcn_ds_read_tr16_b64_v4i16((__attribute__((address_space(3))) s16x4*)(wp + waddr[0])); break;
      case 1: whi[buf][0] = __builtin_amdgcn_ds_read_tr16_b64_v4i16((__attribute__((address_space(3))) s16x4*)(wp + waddr[1])); break;
      case 2: xf[buf][0] = *(const __attribute__((address_space(3))) bf16x8*)(xp); break;
      case 3: wlo[buf][1] = __builtin_amdgcn_ds_read_tr16_b64_v4i16((__attribute__((address_space(3))) s16x4*)(wp + 256 + waddr[0])); break;
      case 4: whi[buf][1] = __builtin_amdgcn_ds_read_tr16_b64_v4i16((__attribute__((address_space(3))) s16x4*)(wp + 256 + waddr[1])); break;
      default: xf[buf][r - 4] = *(const __attribute__((address_space(3))) bf16x8*)(xp + (r - 4) * 2048); break;
    }
  };
  auto mfma_step = [&](int cur, lds_cptr nsb, int nst, bool prefetch) {
    bf16x8 wf0 = __builtin_shufflevector(wlo[cur][0], whi[cur][0], 0, 1, 2, 3, 4, 5, 6, 7);
    bf16x8 wf1 = __builtin_shufflevector(wlo[cur][1], whi[cur][1], 0, 1, 2, 3, 4, 5, 6, 7);
#pragma unroll
    for (int i = 0; i < 10; ++i) {
      const int mb = i >> 1, nb = i & 1;
      acc[mb][nb] = __builtin_amdgcn_mfma_f32_16x16x32_bf16(nb ? wf1 : wf0, xf[cur][mb], acc[mb][nb], 0, 0, 0);
      if (prefetch && i < 9) read_one(nsb, nst, cur ^ 1, i);
      SCHED();
    }
  };

#pragma unroll
  for (int nb = 0; nb < 2; ++nb)
#pragma unroll
    for (int r = 0; r < 4; ++r) asm volatile("" :: "v"(scl[nb][r]), "v"(sft[nb][r]), "v"(scl2[nb][r]), "v"(sft2[nb][r]));
  STAMP(pd);
  __builtin_amdgcn_s_barrier();
  unsigned sbo = 0;
#pragma unroll
  for (int r = 0; r < 9; ++r) read_one((lds_cptr)lds, 0, 0, r);

#pragma unroll
  for (int cc = 0; cc < NCC - 1; ++cc) {
    lds_cptr sb = (lds_cptr)lds + sbo;
#pragma unroll
    for (int st = 0; st < 5; ++st) { SCHED(); mfma_step(st & 1, sb, st + 1, true); }
    sbo = (sbo == 2 * SLOT) ? 0 : sbo + SLOT;
    SCHED();
    WAIT_L0();
    __builtin_amdgcn_s_barrier();
    mfma_step(1, (lds_cptr)lds + sbo, 0, true);
  }
  {
    lds_cptr sb = (lds_cptr)lds + sbo;
#pragma unroll
    for (int st = 0; st < 6; ++st) { SCHED(); mfma_step(st & 1, sb, st + 1, st < 5); }
  }

#pragma unroll
  for (int mb = 0; mb < 5; ++mb) {
    int rho = 80 * wm + 16 * mb + li;
    int s = s0 + rho;
    bool ok = (rho >= 1) && (rho <= OUTR) && (s < S);
    unsigned pk[2][2];
#pragma unroll
    for (int nb = 0; nb < 2; ++nb) {
      float v[4];
#pragma unroll
      for (int r = 0; r < 4; ++r) {
        float a = acc[mb][nb][r];
        float y = __builtin_fmaf(a, scl[nb][r], sft[nb][r]);
        float e = __builtin_amdgcn_exp2f(__builtin_fmaf(a, scl2[nb][r], sft2[nb][r]));
        v[r] = y * __builtin_amdgcn_rcpf(1.0f + e);
      }
      pk[nb][0] = pack_bf16x2(v[0], v[1]);
      pk[nb][1] = pack_bf16x2(v[2], v[3]);
    }
    auto r0v = __builtin_amdgcn_permlane16_swap(pk[0][0], pk[1][0], false, false);
    auto r1v = __builtin_amdgcn_permlane16_swap(pk[0][1], pk[1][1], false, false);
    u32x4 o = {r0v[0], r1v[0], r0v[1], r1v[1]};
    if (ok) {
      unsigned short* p = out + ((long)bb * S + s) * C + n0 + 32 * wn + 16 * (g & 1) + 8 * (g >> 1);
      *(u32x4*)p = o;
    }
  }
}

extern "C" void kernel_launch(void* const* d_in, const int* in_sizes, int n_in,
                              void* d_out, int out_size, void* d_ws, size_t ws_size,
                              hipStream_t stream) {
  const unsigned short* x = (const unsigned short*)d_in[0];
  const unsigned short* w = (const unsigned short*)d_in[1];
  const unsigned short* mean = (const unsigned short*)d_in[2];
  const unsigned short* var = (const unsigned short*)d_in[3];
  const unsigned short* gam = (const unsigned short*)d_in[4];
  const unsigned short* bet = (const unsigned short*)d_in[5];
  unsigned short* out = (unsigned short*)d_out;
  conv_bn_silu_kernel<<<NWG, 512, 0, stream>>>(x, w, mean, var, gam, bet, out);
}
```

```cpp
#include <hip/hip_runtime.h>
#include <hip/hip_bf16.h>
#include <stdint.h>

typedef __attribute__((ext_vector_type(8))) short bf16x8;
typedef __attribute__((ext_vector_type(4))) short s16x4;
typedef __attribute__((ext_vector_type(4))) float f32x4;
typedef __attribute__((ext_vector_type(4))) unsigned u32x4;
typedef __attribute__((address_space(3))) char* lds_ptr;
typedef __attribute__((address_space(3))) const char* lds_cptr;

constexpr int C = 576;
constexpr int S = 2048;
constexpr int ROWB = C * 2;
constexpr int BM = 160;
constexpr int OUTR = BM - 2;
constexpr int MT_PER_B = (S + OUTR - 1) / OUTR;
constexpr int NG = C / 64;
constexpr int NWG = 2 * MT_PER_B * NG;
constexpr int KC = 64;
constexpr int NCC = C / KC;
constexpr int XT_BYTES = BM * 128;
constexpr int WT_BYTES = 64 * 128;
constexpr int SLOT = 3 * WT_BYTES + XT_BYTES;
constexpr int RING = 3;
constexpr int LDS_BYTES = RING * SLOT + 128;
constexpr int LOADS = 5 + 6;

#define WAIT_V(n) asm volatile("s_waitcnt vmcnt(%0)" ::"n"(n) : "memory")
#define WAIT_L0() asm volatile("s_waitcnt lgkmcnt(0)" ::: "memory")
#define SCHED() __builtin_amdgcn_sched_barrier(0)

__device__ __forceinline__ float bf2f(unsigned short u) { return __uint_as_float(((unsigned)u) << 16); }
__device__ __forceinline__ unsigned pack_bf16x2(float lo, float hi) {
  unsigned r; asm("v_cvt_pk_bf16_f32 %0, %1, %2" : "=v"(r) : "v"(lo), "v"(hi)); return r;
}
__device__ __forceinline__ void dma16(u32x4 rsrc, unsigned voff, unsigned soff, unsigned lds_dst) {
  asm volatile("s_mov_b32 m0, %0\n\ts_nop 0\n\tbuffer_load_dwordx4 %1, %2, %3 offen lds"
               :: "s"(lds_dst), "v"(voff), "s"(rsrc), "s"(soff) : "memory");
}
__device__ __forceinline__ u32x4 make_rsrc(const void* base, unsigned bytes) {
  uint64_t a = (uint64_t)base;
  u32x4 r = {(unsigned)a, (unsigned)(a >> 32) & 0xFFFFu, bytes, 0x00020000u};
  r[0] = __builtin_amdgcn_readfirstlane(r[0]); r[1] = __builtin_amdgcn_readfirstlane(r[1]);
  return r;
}


extern "C" __global__ __launch_bounds__(512, 2)
void conv_bn_silu_kernel(const unsigned short* __restrict__ x, const unsigned short* __restrict__ w,
                         const unsigned short* __restrict__ mean, const unsigned short* __restrict__ var,
                         const unsigned short* __restrict__ gam, const unsigned short* __restrict__ bet,
                         unsigned short* __restrict__ out) {
  __shared__ __attribute__((aligned(16))) char smem[LDS_BYTES];
  int bid = blockIdx.x;
  {
    constexpr int q = NWG / 8, r = NWG % 8;
    int xcd = bid & 7, off = bid >> 3;
    bid = (xcd < r ? xcd * (q + 1) : r * (q + 1) + (xcd - r) * q) + off;
  }
  int ngb = bid / (3 * 2 * MT_PER_B), rem = bid % (3 * 2 * MT_PER_B);
  int mt = rem / 3, ngi = rem % 3, ng = ngb * 3 + ngi;
  int bb = mt / MT_PER_B, j = mt % MT_PER_B;
  const int s0 = j * OUTR - 1;
  const int n0 = ng * 64;

  const int tid = threadIdx.x, lane = tid & 63;
  const int wave = __builtin_amdgcn_readfirstlane(tid >> 6);
  const unsigned lds0 = (unsigned)(size_t)(lds_ptr)smem;

  const u32x4 rsX = make_rsrc(x + (long)bb * S * C, S * ROWB);
  const u32x4 rsW = make_rsrc(w + n0, 3 * C * ROWB - n0 * 2);

  if (wave >= 4) {
    const int lw = wave - 4;
    unsigned voffX[5];
    {
      const int srow = s0 + 40 * lw + (lane >> 3);
      const int v0 = srow * ROWB + (((lane & 7) ^ ((lane >> 3) & 6)) << 4);
#pragma unroll
      for (int p = 0; p < 5; ++p) voffX[p] = ((unsigned)(srow + 8 * p) < (unsigned)S) ? (unsigned)(v0 + p * 8 * ROWB) : 0x7FFFFFF0u;
    }
    unsigned voffW[2];
#pragma unroll
    for (int i = 0; i < 2; ++i) {
      int rg = lw * 2 + i;
      int nbp = lane >> 4, idx = lane & 15, pos = idx >> 1, half = idx & 1;
      int row = rg * 8 + (pos ^ ((rg & 1) << 2));
      voffW[i] = (unsigned)(row * ROWB + nbp * 32 + half * 16);
    }
    const unsigned xdst = lds0 + 3 * WT_BYTES + lw * 5 * 1024;
    const unsigned wdst = lds0 + lw * 2 * 1024;
    int ccr = 0;
    const int rot0 = 1 + ((mt + 3 * ngi) & 7);
    unsigned sl = 0;
    auto issue = [&]() {
      const unsigned so = ccr * 128;
#pragma unroll
      for (int p = 0; p < 5; ++p) dma16(rsX, voffX[p], so, xdst + sl + p * 1024);
#pragma unroll
      for (int t = 0; t < 3; ++t)
#pragma unroll
        for (int i = 0; i < 2; ++i) dma16(rsW, voffW[i], (unsigned)(t * C * ROWB) + (unsigned)ccr * (KC * ROWB), wdst + sl + t * WT_BYTES + i * 1024);
      if (true) ccr = (ccr == 0) ? rot0 : ((ccr == NCC - 1) ? 1 : ccr + 1); else ccr = ccr + 1;
      sl = (sl == 2 * SLOT) ? 0 : sl + SLOT;
    };
        issue();
    issue();
        WAIT_V(LOADS);
        __builtin_amdgcn_s_barrier();
    issue();
    for (int jb = 0; jb < NCC - 1; ++jb) {
      if (jb + 2 < NCC) WAIT_V(LOADS); else WAIT_V(0);
      __builtin_amdgcn_s_barrier();
      if (jb + 3 < NCC) issue();
    }
        return;
  }

  const int li = lane & 15, g = lane >> 4;
  const int wm = wave >> 1, wn = wave & 1;
  lds_ptr lds = (lds_ptr)smem;

  float scl[2][4], sft[2][4], scl2[2][4], sft2[2][4];
#pragma unroll
  for (int nb = 0; nb < 2; ++nb) {
    int n = n0 + 32 * wn + 16 * nb + 4 * g;
    s16x4 mv = *(const s16x4*)(mean + n), vv = *(const s16x4*)(var + n), gv = *(const s16x4*)(gam + n), bv = *(const s16x4*)(bet + n);
#pragma unroll
    for (int r = 0; r < 4; ++r) {
      float sc = bf2f((unsigned short)gv[r]) * __builtin_amdgcn_rsqf(bf2f((unsigned short)vv[r]) + 1e-5f);
      float sf = bf2f((unsigned short)bv[r]) - bf2f((unsigned short)mv[r]) * sc;
      scl[nb][r] = sc; sft[nb][r] = sf;
      scl2[nb][r] = sc * -1.4426950408889634f; sft2[nb][r] = sf * -1.4426950408889634f;
    }
  }

  unsigned xaddr[3][2];
#pragma unroll
  for (int t = 0; t < 3; ++t)
#pragma unroll
    for (int ks = 0; ks < 2; ++ks) {
      int rho = 80 * wm + li + t - 1, c = 4 * ks + g;
      xaddr[t][ks] = (unsigned)(3 * WT_BYTES + rho * 128 + ((c ^ (rho & 6)) << 4));
    }
  unsigned waddr[2];
#pragma unroll
  for (int tt = 0; tt < 2; ++tt)
    waddr[tt] = (unsigned)(g * 1024 + wn * 512 + ((tt ^ (g & 1)) * 128) + ((li >> 2) * 32) + ((li & 3) * 8));

  f32x4 acc[5][2];
#pragma unroll
  for (int mb = 0; mb < 5; ++mb)
#pragma unroll
    for (int nb = 0; nb < 2; ++nb) acc[mb][nb] = (f32x4){0.f, 0.f, 0.f, 0.f};

  bf16x8 xf[2][5];
  s16x4 wlo[2][2], whi[2][2];
  auto read_one = [&](lds_cptr sb, int st, int buf, int r) {
    const int t = st >> 1, ks = st & 1;
    lds_cptr wp = sb + t * WT_BYTES + ks * 4096;
    lds_cptr xp = sb + xaddr[t][ks];
    switch (r) {
      case 0: wlo[buf][0] = __builtin_amdgcn_ds_read_tr16_b64_v4i16((__attribute__((address_space(3))) s16x4*)(wp + waddr[0])); break;
      case 1: whi[buf][0] = __builtin_amdgcn_ds_read_tr16_b64_v4i16((__attribute__((address_space(3))) s16x4*)(wp + waddr[1])); break;
      case 2: xf[buf][0] = *(const __attribute__((address_space(3))) bf16x8*)(xp); break;
      case 3: wlo[buf][1] = __builtin_amdgcn_ds_read_tr16_b64_v4i16((__attribute__((address_space(3))) s16x4*)(wp + 256 + waddr[0])); break;
      case 4: whi[buf][1] = __builtin_amdgcn_ds_read_tr16_b64_v4i16((__attribute__((address_space(3))) s16x4*)(wp + 256 + waddr[1])); break;
      default: xf[buf][r - 4] = *(const __attribute__((address_space(3))) bf16x8*)(xp + (r - 4) * 2048); break;
    }
  };
  auto mfma_step = [&](int cur, lds_cptr nsb, int nst, bool prefetch) {
    bf16x8 wf0 = __builtin_shufflevector(wlo[cur][0], whi[cur][0], 0, 1, 2, 3, 4, 5, 6, 7);
    bf16x8 wf1 = __builtin_shufflevector(wlo[cur][1], whi[cur][1], 0, 1, 2, 3, 4, 5, 6, 7);
#pragma unroll
    for (int i = 0; i < 10; ++i) {
      const int mb = i >> 1, nb = i & 1;
      acc[mb][nb] = __builtin_amdgcn_mfma_f32_16x16x32_bf16(nb ? wf1 : wf0, xf[cur][mb], acc[mb][nb], 0, 0, 0);
      if (prefetch && i < 9) read_one(nsb, nst, cur ^ 1, i);
      SCHED();
    }
  };

#pragma unroll
  for (int nb = 0; nb < 2; ++nb)
#pragma unroll
    for (int r = 0; r < 4; ++r) asm volatile("" :: "v"(scl[nb][r]), "v"(sft[nb][r]), "v"(scl2[nb][r]), "v"(sft2[nb][r]));
    __builtin_amdgcn_s_barrier();
  unsigned sbo = 0;
#pragma unroll
  for (int r = 0; r < 9; ++r) read_one((lds_cptr)lds, 0, 0, r);

#pragma unroll
  for (int cc = 0; cc < NCC - 1; ++cc) {
    lds_cptr sb = (lds_cptr)lds + sbo;
#pragma unroll
    for (int st = 0; st < 5; ++st) { SCHED(); mfma_step(st & 1, sb, st + 1, true); }
    sbo = (sbo == 2 * SLOT) ? 0 : sbo + SLOT;
    SCHED();
    WAIT_L0();
    __builtin_amdgcn_s_barrier();
    mfma_step(1, (lds_cptr)lds + sbo, 0, true);
  }
  {
    lds_cptr sb = (lds_cptr)lds + sbo;
#pragma unroll
    for (int st = 0; st < 6; ++st) { SCHED(); mfma_step(st & 1, sb, st + 1, st < 5); }
  }

#pragma unroll
  for (int mb = 0; mb < 5; ++mb) {
    int rho = 80 * wm + 16 * mb + li;
    int s = s0 + rho;
    bool ok = (rho >= 1) && (rho <= OUTR) && (s < S);
    unsigned pk[2][2];
#pragma unroll
    for (int nb = 0; nb < 2; ++nb) {
      float v[4];
#pragma unroll
      for (int r = 0; r < 4; ++r) {
        float a = acc[mb][nb][r];
        float y = __builtin_fmaf(a, scl[nb][r], sft[nb][r]);
        float e = __builtin_amdgcn_exp2f(__builtin_fmaf(a, scl2[nb][r], sft2[nb][r]));
        v[r] = y * __builtin_amdgcn_rcpf(1.0f + e);
      }
      pk[nb][0] = pack_bf16x2(v[0], v[1]);
      pk[nb][1] = pack_bf16x2(v[2], v[3]);
    }
    auto r0v = __builtin_amdgcn_permlane16_swap(pk[0][0], pk[1][0], false, false);
    auto r1v = __builtin_amdgcn_permlane16_swap(pk[0][1], pk[1][1], false, false);
    u32x4 o = {r0v[0], r1v[0], r0v[1], r1v[1]};
    if (ok) {
      unsigned short* p = out + ((long)bb * S + s) * C + n0 + 32 * wn + 16 * (g & 1) + 8 * (g >> 1);
      *(u32x4*)p = o;
    }
  }
}

extern "C" void kernel_launch(void* const* d_in, const int* in_sizes, int n_in,
                              void* d_out, int out_size, void* d_ws, size_t ws_size,
                              hipStream_t stream) {
  const unsigned short* x = (const unsigned short*)d_in[0];
  const unsigned short* w = (const unsigned short*)d_in[1];
  const unsigned short* mean = (const unsigned short*)d_in[2];
  const unsigned short* var = (const unsigned short*)d_in[3];
  const unsigned short* gam = (const unsigned short*)d_in[4];
  const unsigned short* bet = (const unsigned short*)d_in[5];
  unsigned short* out = (unsigned short*)d_out;
  conv_bn_silu_kernel<<<NWG, 512, 0, stream>>>(x, w, mean, var, gam, bet, out);
}
```
